```python
import torch

def _softmax(x, axis=-1):
    return torch.softmax(x, dim=axis)
def _gelu(x):
    return torch.nn.functional.gelu(x, approximate="tanh")
def _silu(x):
    return torch.nn.functional.silu(x)
def _sigmoid(x):
    return torch.sigmoid(x)
def _layernorm(x, axis=-1, eps=1e-5):
    m = x.mean(dim=axis, keepdim=True)
    v = x.var(dim=axis, keepdim=True, unbiased=False)
    return (x - m) / torch.sqrt(v + eps)
def _scatter_add(tgt, idx, src, axis=0):
    return tgt.clone().index_add_(axis, idx, src)
def _segment_sum(src, seg, n):
    out = torch.zeros((n,) + src.shape[1:], dtype=src.dtype)
    return out.index_add_(0, seg, src)
def _pad(x, pw):
    flat = tuple(v for pair in reversed(pw) for v in pair)
    return torch.nn.functional.pad(x, flat)

kernel_name = 'concat_heads__gqa_block_cached__gqa_block_cached__near_ridge'

def setup_inputs():

    _w = (lambda a: (lambda t: t.to(torch.bfloat16) if t.is_floating_point() else t)(torch.as_tensor(a)))
    def _A_setup():
        B, Sq, D = 16, 96, 512
        Hq, Hkv, Sk, Dh = 32, 32, 512, 128
        assert Hq % Hkv == 0, (Hq, Hkv)
        return {
            "x": _w((lambda *s: torch.randn(*s, dtype=torch.bfloat16) * 0.8)(B, Sq, D)),
            "k_cache": _w((lambda *s: torch.randn(*s, dtype=torch.bfloat16) * 0.8)(B, Hkv, Sk, Dh)),
            "v_cache": _w((lambda *s: torch.randn(*s, dtype=torch.bfloat16) * 0.05)(B, Hkv, Sk, Dh)),
            "wq": _w((lambda *s: torch.randn(*s, dtype=torch.bfloat16) * 0.8)(D, Hq * Dh) * (D ** -0.5)),
            "wo": _w((lambda *s: torch.randn(*s, dtype=torch.bfloat16) * 0.05)(Hq * Dh, D) * ((Hq * Dh) ** -0.5)),
        }
    _A_in = _A_setup()

    def _B_setup():
        B, Sq, D = 16, 96, 512
        Hq, Hkv, Sk, Dh = 32, 32, 512, 128
        assert Hq % Hkv == 0, (Hq, Hkv)
        return {
            "x": _w((lambda *s: torch.randn(*s, dtype=torch.bfloat16) * 0.8)(B, Sq, D)),
            "k_cache": _w((lambda *s: torch.randn(*s, dtype=torch.bfloat16) * 0.8)(B, Hkv, Sk, Dh)),
            "v_cache": _w((lambda *s: torch.randn(*s, dtype=torch.bfloat16) * 0.05)(B, Hkv, Sk, Dh)),
            "wq": _w((lambda *s: torch.randn(*s, dtype=torch.bfloat16) * 0.8)(D, Hq * Dh) * (D ** -0.5)),
            "wo": _w((lambda *s: torch.randn(*s, dtype=torch.bfloat16) * 0.05)(Hq * Dh, D) * ((Hq * Dh) ** -0.5)),
        }
    _B_in = _B_setup()

    return {
        "A_x": _A_in["x"],
        "A_k_cache": _A_in["k_cache"],
        "A_v_cache": _A_in["v_cache"],
        "A_wq": _A_in["wq"],
        "A_wo": _A_in["wo"],
        "B_x": _B_in["x"],
        "B_k_cache": _B_in["k_cache"],
        "B_v_cache": _B_in["v_cache"],
        "B_wq": _B_in["wq"],
        "B_wo": _B_in["wo"],
    }

def reference(A_x, A_k_cache, A_v_cache, A_wq, A_wo, B_x, B_k_cache, B_v_cache, B_wq, B_wo):
    def _A_ref(x, k_cache, v_cache, wq, wo):
        B, Sq, D = x.shape
        Hq, Dh = 32, 128
        Hkv = k_cache.shape[1]
        rep = Hq // Hkv
        q = (lambda x, *d: x.permute(*d))((x @ wq).reshape(B, Sq, Hq, Dh), 0, 2, 1, 3)
        k = (lambda x, n, axis: torch.repeat_interleave(x, n, dim=axis))(k_cache, rep, axis=1)
        v = (lambda x, n, axis: torch.repeat_interleave(x, n, dim=axis))(v_cache, rep, axis=1)
        scores = torch.einsum("bhqd,bhkd->bhqk", q, k) * (Dh ** -0.5)
        p = _softmax(scores, axis=-1)
        o = torch.einsum("bhqk,bhkd->bhqd", p, v)
        return (lambda x, *d: x.permute(*d))(o, 0, 2, 1, 3).reshape(B, Sq, Hq * Dh) @ wo

    def _B_ref(x, k_cache, v_cache, wq, wo):
        B, Sq, D = x.shape
        Hq, Dh = 32, 128
        Hkv = k_cache.shape[1]
        rep = Hq // Hkv
        q = (lambda x, *d: x.permute(*d))((x @ wq).reshape(B, Sq, Hq, Dh), 0, 2, 1, 3)
        k = (lambda x, n, axis: torch.repeat_interleave(x, n, dim=axis))(k_cache, rep, axis=1)
        v = (lambda x, n, axis: torch.repeat_interleave(x, n, dim=axis))(v_cache, rep, axis=1)
        scores = torch.einsum("bhqd,bhkd->bhqk", q, k) * (Dh ** -0.5)
        p = _softmax(scores, axis=-1)
        o = torch.einsum("bhqk,bhkd->bhqd", p, v)
        return (lambda x, *d: x.permute(*d))(o, 0, 2, 1, 3).reshape(B, Sq, Hq * Dh) @ wo

    a = _A_ref(A_x, A_k_cache, A_v_cache, A_wq, A_wo)
    b = _B_ref(B_x, B_k_cache, B_v_cache, B_wq, B_wo)
    return torch.concatenate([a, b], axis=-1)
```

```cpp
#include <hip/hip_runtime.h>
#include <hip/hip_bf16.h>
typedef __hip_bfloat16 bf16;
constexpr int B=16,SQ=96,D=512,H=32,SK=512,DH=128;
__global__ void k_q(const bf16* x,const bf16* wq,float* q){
  int col = blockIdx.x*256+threadIdx.x; int row = blockIdx.y;
  float acc=0; for(int k=0;k<D;++k) acc += __bfloat162float(x[row*D+k])*__bfloat162float(wq[(size_t)k*4096+col]);
  q[(size_t)row*4096+col]=__bfloat162float(__float2bfloat16(acc));
}
__global__ void k_attn(const float* q,const bf16* kc,const bf16* vc,float* o){
  int s=blockIdx.x,h=blockIdx.y,b=blockIdx.z; __shared__ float qs[DH]; __shared__ float p[SK]; __shared__ float red[128];
  int t=threadIdx.x; qs[t]=q[(size_t)(b*SQ+s)*4096+h*DH+t]; __syncthreads();
  const bf16* K=kc+(size_t)(b*H+h)*SK*DH; const bf16* V=vc+(size_t)(b*H+h)*SK*DH;
  float mx=-1e30f;
  for(int j=t;j<SK;j+=128){ float a=0; for(int d=0;d<DH;++d) a+=qs[d]*__bfloat162float(K[j*DH+d]); a*=0.08838834764831845f; p[j]=a; mx=fmaxf(mx,a);}
  red[t]=mx; __syncthreads(); for(int o2=64;o2>0;o2>>=1){ if(t<o2) red[t]=fmaxf(red[t],red[t+o2]); __syncthreads(); } mx=red[0]; __syncthreads();
  float sum=0; for(int j=t;j<SK;j+=128){ float e=__expf(p[j]-mx); p[j]=e; sum+=e;} red[t]=sum; __syncthreads();
  for(int o2=64;o2>0;o2>>=1){ if(t<o2) red[t]+=red[t+o2]; __syncthreads(); } sum=red[0];
  float acc=0; for(int j=0;j<SK;++j) acc+=p[j]*__bfloat162float(V[j*DH+t]);
  o[(size_t)(b*SQ+s)*4096+h*DH+t]=__bfloat162float(__float2bfloat16(acc/sum));
}
__global__ void k_o(const float* o,const bf16* wo,bf16* out,int coff){
  int col=blockIdx.x*256+threadIdx.x; int row=blockIdx.y;
  float acc=0; for(int k=0;k<4096;++k) acc+=o[(size_t)row*4096+k]*__bfloat162float(wo[(size_t)k*512+col]);
  out[(size_t)row*1024+coff+col]=__float2bfloat16(acc);
}
extern "C" void kernel_launch(void* const* d_in,const int* in_sizes,int n_in,void* d_out,int out_size,void* d_ws,size_t ws_size,hipStream_t stream){
  float* q=(float*)d_ws; float* o=q+(size_t)1536*4096;
  for(int br=0;br<2;++br){
    const bf16* x=(const bf16*)d_in[br*5+0]; const bf16* kc=(const bf16*)d_in[br*5+1]; const bf16* vc=(const bf16*)d_in[br*5+2];
    const bf16* wq=(const bf16*)d_in[br*5+3]; const bf16* wo=(const bf16*)d_in[br*5+4];
    k_q<<<dim3(16,1536),256,0,stream>>>(x,wq,q);
    k_attn<<<dim3(SQ,H,B),128,0,stream>>>(q,kc,vc,o);
    k_o<<<dim3(2,1536),256,0,stream>>>(o,wo,(bf16*)d_out,br*512);
  }
}
```

```cpp
#include <hip/hip_runtime.h>
#include <hip/hip_bf16.h>
#include <stdint.h>

#define PRIO_HI 1
#define PRIO_LD 0
namespace fk {
typedef __attribute__((ext_vector_type(8))) __bf16 bf16x8;
typedef __attribute__((ext_vector_type(2))) __bf16 bf16x2;
typedef __attribute__((ext_vector_type(4))) short s16x4;
typedef __attribute__((ext_vector_type(8))) short s16x8;
typedef __attribute__((ext_vector_type(2))) float f32x2;
typedef __attribute__((ext_vector_type(4))) float f32x4;
typedef __attribute__((ext_vector_type(16))) float f32x16;
typedef __attribute__((ext_vector_type(4))) unsigned u32x4;
typedef __attribute__((ext_vector_type(2))) unsigned u32x2;
typedef unsigned long long u64;
#define LAS __attribute__((address_space(3)))
#define GAS __attribute__((address_space(1)))
#define DI __device__ __forceinline__

constexpr unsigned LDS_KV = 0;
constexpr unsigned LDS_X1 = 0;
constexpr unsigned LDS_X2 = 65536;
constexpr unsigned LDS_WQ = 98304;
constexpr unsigned LDS_R  = 65536;
constexpr unsigned LDS_WO = 114688;
constexpr unsigned LDS_BYTES = 163840;
constexpr float QSCALE = 0.08838834764831845f * 1.4426950408889634f;
constexpr float THR = 8.0f;
constexpr size_t WS_FLAG = 0;
constexpr size_t WS_GEN  = 32768;
constexpr size_t WS_STAMP = 40960;
constexpr size_t WS_SLAB = 2097152;

DI unsigned pk2(float a, float b) { f32x2 v = {a, b}; bf16x2 r = __builtin_convertvector(v, bf16x2); return __builtin_bit_cast(unsigned, r); }
DI bf16x8 mk8(unsigned a, unsigned b, unsigned c, unsigned d) { u32x4 w = {a, b, c, d}; return __builtin_bit_cast(bf16x8, w); }
DI bf16x8 lds128(unsigned a) { return *(const LAS bf16x8*)a; }
DI bf16x8 ldstr(unsigned a, unsigned off2) {
  s16x4 lo = __builtin_amdgcn_ds_read_tr16_b64_v4i16((LAS s16x4*)a);
  s16x4 hi = __builtin_amdgcn_ds_read_tr16_b64_v4i16((LAS s16x4*)(a + off2));
  s16x8 v = __builtin_shufflevector(lo, hi, 0, 1, 2, 3, 4, 5, 6, 7);
  return __builtin_bit_cast(bf16x8, v);
}
#define MFMA16(a, b, c) __builtin_amdgcn_mfma_f32_16x16x32_bf16((a), (b), (c), 0, 0, 0)
#define MFMA32(a, b, c) __builtin_amdgcn_mfma_f32_32x32x16_bf16((a), (b), (c), 0, 0, 0)

#define FK_DMA4(NT) \
  asm volatile("s_mov_b32 m0, %0\n\ts_nop 3\n\t" \
               "buffer_load_dwordx4 %1, %5, %6 offen " NT "lds\n\t" \
               "buffer_load_dwordx4 %2, %5, %6 offen offset:1024 " NT "lds\n\t" \
               "buffer_load_dwordx4 %3, %5, %6 offen offset:2048 " NT "lds\n\t" \
               "buffer_load_dwordx4 %4, %5, %6 offen offset:3072 " NT "lds" \
               :: "s"(lds), "v"(v0), "v"(v1), "v"(v2), "v"(v3), "s"(rs), "s"(soff) : "memory")
DI void dma4(__amdgpu_buffer_rsrc_t rs, unsigned v0, unsigned v1, unsigned v2, unsigned v3, unsigned soff, unsigned lds) { FK_DMA4(""); }
DI void dma4_nt(__amdgpu_buffer_rsrc_t rs, unsigned v0, unsigned v1, unsigned v2, unsigned v3, unsigned soff, unsigned lds) { FK_DMA4("nt "); }
#undef FK_DMA4
DI void dma3(__amdgpu_buffer_rsrc_t rs, unsigned v0, unsigned v1, unsigned v2, unsigned soff, unsigned lds) {
  asm volatile("s_mov_b32 m0, %0\n\ts_nop 3\n\t"
               "buffer_load_dwordx4 %1, %4, %5 offen lds\n\t"
               "buffer_load_dwordx4 %2, %4, %5 offen offset:1024 lds\n\t"
               "buffer_load_dwordx4 %3, %4, %5 offen offset:2048 lds"
               :: "s"(lds), "v"(v0), "v"(v1), "v"(v2), "s"(rs), "s"(soff) : "memory");
}
template <int N> DI void wait_vmc() { asm volatile("s_waitcnt vmcnt(%0)" :: "n"(N) : "memory"); }
DI unsigned lane_now() {
  unsigned l; asm volatile("v_mbcnt_lo_u32_b32 %0, -1, 0\n\tv_mbcnt_hi_u32_b32 %0, -1, %0" : "=v"(l)); return l;
}
DI void barrier() {
  __builtin_amdgcn_sched_barrier(0);
  __builtin_amdgcn_s_waitcnt(0xC07F);
  asm volatile("" ::: "memory");
  __builtin_amdgcn_s_barrier();
  asm volatile("" ::: "memory");
  __builtin_amdgcn_sched_barrier(0);
}
DI float max3(float a, float b, float c) { return __builtin_elementwise_maximum(__builtin_elementwise_maximum(a, b), c); }
#define WC(k) case k: wait_vmc<k>(); return;
DI void wait_k(int n) {
  switch (n) {
    WC(0) WC(1) WC(2) WC(3) WC(4) WC(5) WC(6) WC(7) WC(8) WC(9) WC(10) WC(11) WC(12) WC(13) WC(14) WC(15)
    WC(16) WC(17) WC(18) WC(19) WC(20) WC(21) WC(22) WC(23) WC(24) WC(25) WC(26) WC(27) WC(28) WC(29) WC(30) WC(31)
    WC(32) WC(33) WC(34) WC(35) WC(36) WC(37) WC(38) WC(39) WC(40)
    default: wait_vmc<40>(); return;
  }
}
#undef WC

#define STAMP(i) do {} while (0)
#define BSTAMP(i) do {} while (0)
#define BSTAMP2(i) do {} while (0)
#define FSTAMP(i) do {} while (0)

template <int NM, int NR> DI void mix_mfma_reads() {
#pragma unroll
  for (int i = 0; i < NR; ++i) {
    __builtin_amdgcn_sched_group_barrier(0x008, 1, 0);
    __builtin_amdgcn_sched_group_barrier(0x002, 1, 0);
    __builtin_amdgcn_sched_group_barrier(0x100, 1, 0);
  }
  __builtin_amdgcn_sched_group_barrier(0x008, NM - NR, 0);
}
struct QFr { bf16x8 a[8]; bf16x8 b[3]; };
DI void q_read(QFr& f, unsigned wa, unsigned xa) {
#pragma unroll
  for (int c = 0; c < 8; ++c) f.a[c] = ldstr(wa ^ (unsigned)(c << 5), 1024);
#pragma unroll
  for (int qt = 0; qt < 3; ++qt) f.b[qt] = lds128(xa + qt * 2048);
}
template <class H> DI void q_mma_h(f32x4 (&acc)[8][3], const QFr& f, H hook) {
#pragma unroll
  for (int c = 0; c < 8; ++c) {
#pragma unroll
    for (int qt = 0; qt < 3; ++qt) acc[c][qt] = MFMA16(f.a[c], f.b[qt], acc[c][qt]);
    if (c & 1) { __builtin_amdgcn_sched_barrier(0); hook(c >> 1); __builtin_amdgcn_sched_barrier(0); }
  }
}
DI void q_mma(f32x4 (&acc)[8][3], const QFr& f) {
#pragma unroll
  for (int c = 0; c < 8; ++c)
#pragma unroll
    for (int qt = 0; qt < 3; ++qt) acc[c][qt] = MFMA16(f.a[c], f.b[qt], acc[c][qt]);
}
DI void q_finish(const f32x4 (&acc)[8][3], bf16x8 (&qf)[3][4]) {
#pragma unroll
  for (int qt = 0; qt < 3; ++qt)
#pragma unroll
    for (int s = 0; s < 4; ++s) {
      const f32x4 ta = acc[2 * s][qt], tb = acc[2 * s + 1][qt];
      unsigned a0 = pk2(ta[0] * QSCALE, ta[1] * QSCALE), a1 = pk2(ta[2] * QSCALE, ta[3] * QSCALE);
      unsigned b0 = pk2(tb[0] * QSCALE, tb[1] * QSCALE), b1 = pk2(tb[2] * QSCALE, tb[3] * QSCALE);
      auto r0 = __builtin_amdgcn_permlane32_swap(a0, b0, false, false);
      auto r1 = __builtin_amdgcn_permlane32_swap(a1, b1, false, false);
      auto s0 = __builtin_amdgcn_permlane16_swap(r0[0], r0[1], false, false);
      auto s1 = __builtin_amdgcn_permlane16_swap(r1[0], r1[1], false, false);
      qf[qt][s] = mk8(s0[0], s1[0], s0[1], s1[1]);
    }
}

struct Attn {
  f32x4 o[8][3];
  f32x4 s[2][3];
  float m[3], l[3];
};
DI float xmax4(float v) {
  v = __builtin_elementwise_maximum(v, __shfl_xor(v, 16)); return __builtin_elementwise_maximum(v, __shfl_xor(v, 32));
}
DI void k_step(Attn& at, const bf16x8 (&qf)[3][4], unsigned ka, float thr, float lo) {
#pragma unroll
  for (int kt = 0; kt < 2; ++kt) {
    bf16x8 kf[4];
#pragma unroll
    for (int ks = 0; ks < 4; ++ks) kf[ks] = lds128((ka ^ (unsigned)(ks << 6)) + kt * 4096);
#pragma unroll
    for (int qt = 0; qt < 3; ++qt) {
      const float nm = -at.m[qt];
      f32x4 c = {nm, nm, nm, nm};
#pragma unroll
      for (int ks = 0; ks < 4; ++ks) c = MFMA16(kf[ks], qf[qt][ks], c);
      at.s[kt][qt] = c;
    }
  }
  float mx[3];
#pragma unroll
  for (int qt = 0; qt < 3; ++qt) {
    const f32x4 a = at.s[0][qt], b = at.s[1][qt];
    mx[qt] = max3(max3(max3(a[0], a[1], a[2]), a[3], b[0]), max3(b[1], b[2], b[3]), a[0]);
  }
  const float mall = max3(mx[0], mx[1], mx[2]);
  if (__any(mall > thr)) {
#pragma unroll
    for (int qt = 0; qt < 3; ++qt) {
      const float d = __builtin_elementwise_maximum(xmax4(mx[qt]), lo);
      const float f = __builtin_amdgcn_exp2f(-__builtin_elementwise_maximum(d, 0.f));
      at.l[qt] *= f;
#pragma unroll
      for (int dt = 0; dt < 8; ++dt) at.o[dt][qt] *= f;
      at.m[qt] += d;
      at.s[0][qt] -= d; at.s[1][qt] -= d;
    }
  }
}
DI void v_step(Attn& at, unsigned va) {
  bf16x8 vf[8];
#pragma unroll
  for (int dt = 0; dt < 8; ++dt) vf[dt] = ldstr(va ^ (unsigned)(dt << 5), 4096);
  bf16x8 p[3];
#pragma unroll
  for (int qt = 0; qt < 3; ++qt) {
    float e[8];
#pragma unroll
    for (int i = 0; i < 4; ++i) { e[i] = __builtin_amdgcn_exp2f(at.s[0][qt][i]); e[4 + i] = __builtin_amdgcn_exp2f(at.s[1][qt][i]); }
    at.l[qt] += ((e[0] + e[1]) + (e[2] + e[3])) + ((e[4] + e[5]) + (e[6] + e[7]));
    p[qt] = mk8(pk2(e[0], e[1]), pk2(e[2], e[3]), pk2(e[4], e[5]), pk2(e[6], e[7]));
  }
#pragma unroll
  for (int qt = 0; qt < 3; ++qt)
#pragma unroll
    for (int dt = 0; dt < 8; ++dt) at.o[dt][qt] = MFMA16(vf[dt], p[qt], at.o[dt][qt]);
}
DI void attn_store(Attn& at, unsigned ra) {
#pragma unroll
  for (int qt = 0; qt < 3; ++qt) {
    float l = at.l[qt]; l += __shfl_xor(l, 16); l += __shfl_xor(l, 32);
    const float inv = 1.0f / l;
#pragma unroll
    for (int dt = 0; dt < 8; ++dt) {
      const f32x4 v = at.o[dt][qt] * inv;
      u32x2 w = {pk2(v[0], v[1]), pk2(v[2], v[3])};
      *(LAS u32x2*)((ra ^ (unsigned)(dt << 5)) + qt * 8192) = w;
    }
  }
}

__global__ __launch_bounds__(512, 2) void fused_kernel(
    const char* __restrict__ xA, const char* __restrict__ kA, const char* __restrict__ vA, const char* __restrict__ wqA, const char* __restrict__ woA,
    const char* __restrict__ xB, const char* __restrict__ kB, const char* __restrict__ vB, const char* __restrict__ wqB, const char* __restrict__ woB,
    char* __restrict__ out, char* __restrict__ ws) {
  __shared__ __attribute__((aligned(1024))) char smem[LDS_BYTES];
  const unsigned L0 = (unsigned)(uintptr_t)(LAS char*)smem;
  const int tid = threadIdx.x, lane = tid & 63;
  const int wid = __builtin_amdgcn_readfirstlane(tid >> 6);
  const int bid = blockIdx.x, xcd = bid & 7, jj = bid >> 3;
  const int br = xcd >> 2, g = (xcd & 3) * 2 + (jj & 1), b = jj >> 1;
  const int grp = wid >> 2, lw = wid & 3, hh = lw >> 1, jq = lw & 1;
  const int rot = (bid * 5 + (bid >> 3)) & 15, rotq = 2 * ((b + 4 * (g & 1)) & 7), roto = b;
  const __amdgpu_buffer_rsrc_t rsK = __builtin_amdgcn_make_buffer_rsrc((void*)((br ? kB : kA) + (size_t)(b * 32 + g * 4) * 131072), 0, 524288, 0x00020000);
  const __amdgpu_buffer_rsrc_t rsV = __builtin_amdgcn_make_buffer_rsrc((void*)((br ? vB : vA) + (size_t)(b * 32 + g * 4) * 131072), 0, 524288, 0x00020000);
  const __amdgpu_buffer_rsrc_t rsQ = __builtin_amdgcn_make_buffer_rsrc((void*)((br ? wqB : wqA) + g * 1024), 0, 4194304 - g * 1024, 0x00020000);
  const __amdgpu_buffer_rsrc_t rsO = __builtin_amdgcn_make_buffer_rsrc((void*)((br ? woB : woA) + (size_t)g * 524288), 0, 524288, 0x00020000);
  const __amdgpu_buffer_rsrc_t rsX = __builtin_amdgcn_make_buffer_rsrc((void*)((br ? xB : xA) + (size_t)b * 98304), 0, 98304, 0x00020000);
  const int l4 = lane >> 4, l15 = lane & 15, q2 = (lane >> 2) & 3, p2 = lane & 3;
  STAMP(0);

  unsigned bK, bV, bO;
  auto set_bases = [&]() {
    const unsigned ln = lane_now(), n4 = ln >> 4, n15 = ln & 15;
    bK = n4 * 256 + 16 * (n15 ^ n4); bV = n4 * 256 + 16 * (n15 ^ (2 * n4)); bO = n4 * 1024 + 16 * (n15 ^ (n4 << 2));
  };
  const unsigned kvq = (unsigned)(hh * 131072 + jq * 4096), kvl = L0 + LDS_KV + lw * 4096;
  auto ld_k = [&](int i) {
    const int t = (((i >> 1) & 15) + rot) & 15;
    unsigned v = bK; asm volatile("" : "+v"(v));
    dma4_nt(rsK, v, v ^ 64, v ^ 128, v ^ 192, (unsigned)((i >> 5) * 262144 + t * 8192) + kvq, kvl + (i & 3) * 16384);
  };
  auto ld_v = [&](int i) {
    const int t = (((i >> 1) & 15) + rot) & 15;
    unsigned v = bV; asm volatile("" : "+v"(v));
    dma4_nt(rsV, v, v ^ 128, v, v ^ 128, (unsigned)((i >> 5) * 262144 + t * 8192) + kvq, kvl + (i & 3) * 16384);
  };
  auto ld_kv = [&](int i) { if (i & 1) ld_v(i); else ld_k(i); };
  auto ld_wo = [&](int ks) {
    const int kk = (ks + roto) & 15;
    unsigned v = bO; asm volatile("" : "+v"(v));
    dma4(rsO, v, v + 3072, v + 2 * 3072, v + 3 * 3072, (unsigned)(((ks >> 4) * 256 + kk * 16) * 1024 + lw * 256), L0 + LDS_WO + lw * 12288 + (ks % 3) * 4096);
  };
  const unsigned aW = L0 + LDS_WQ + hh * 8192 + (8 * l4 + q2) * 256 + 16 * ((p2 >> 1) ^ (q2 << 2) ^ ((l4 & 1) << 1)) + 8 * (p2 & 1);
  const unsigned aX = L0 + (48 * jq + l15) * 128 + 16 * (l4 ^ (l15 >> 1));
  const unsigned aK = L0 + LDS_KV + hh * 8192 + l15 * 256 + 16 * (l4 ^ l15);
  const unsigned aV = L0 + LDS_KV + hh * 8192 + (4 * l4 + q2) * 256 + 16 * ((p2 >> 1) ^ (((4 * l4 + q2) & 7) << 1)) + 8 * (p2 & 1);
  const unsigned aRw = L0 + LDS_R + (48 * jq + l15) * 512 + hh * 256 + 16 * ((l4 >> 1) ^ l15) + 8 * (l4 & 1);

  auto xpos = [](int x) { return (unsigned)(LDS_X1 + ((x + 5) & 7) * 12288); };

  if (grp == 0) {
    u64 gen = 0;
    __builtin_amdgcn_s_setprio(PRIO_HI);
    if (wid == 0) gen = __hip_atomic_load((GAS u64*)(ws + WS_GEN + (size_t)(br * 16 + b) * 128), __ATOMIC_RELAXED, __HIP_MEMORY_SCOPE_AGENT);
    bf16x8 qf[3][4];
    {
      f32x4 acc[8][3];
#pragma unroll
      for (int c = 0; c < 8; ++c)
#pragma unroll
        for (int qt = 0; qt < 3; ++qt) acc[c][qt] = f32x4{0.f, 0.f, 0.f, 0.f};
      QFr f0, f1;
      barrier();
      FSTAMP(16);
      q_read(f0, aW, aX + xpos(0));
      barrier();
      FSTAMP(17);
      for (int p = 1; p < 15; p += 2) {
        q_read(f1, aW + (p & 3) * 16384, (aX ^ 64u) + xpos(p >> 1));
        q_mma(acc, f0);
        mix_mfma_reads<24, 19>();
        barrier();
        FSTAMP(17 + p);
        q_read(f0, aW + ((p + 1) & 3) * 16384, aX + xpos((p + 1) >> 1));
        q_mma(acc, f1);
        mix_mfma_reads<24, 19>();
        barrier();
        FSTAMP(18 + p);
      }
      q_read(f1, aW + 3 * 16384, (aX ^ 64u) + xpos(7));
      q_mma(acc, f0);
      mix_mfma_reads<24, 19>();
      barrier();
      FSTAMP(32);
      q_mma(acc, f1);
      q_finish(acc, qf);
      barrier();
      FSTAMP(33);
    }
    STAMP(1);
    Attn at;
#pragma unroll
    for (int dt = 0; dt < 8; ++dt)
#pragma unroll
      for (int qt = 0; qt < 3; ++qt) at.o[dt][qt] = f32x4{0.f, 0.f, 0.f, 0.f};
#pragma unroll
    for (int qt = 0; qt < 3; ++qt) { at.m[qt] = 0.f; at.l[qt] = 0.f; }
    for (int t = 0; t < 16; ++t) {
      k_step(at, qf, aK + ((2 * t) & 3) * 16384, t ? THR : -__builtin_inff(), t ? 0.f : -__builtin_inff());
      FSTAMP(40 + 4 * t);
      barrier();
      FSTAMP(41 + 4 * t);
      v_step(at, aV + ((2 * t + 1) & 3) * 16384);
      FSTAMP(42 + 4 * t);
      barrier();
      FSTAMP(43 + 4 * t);
    }
    STAMP(2);
    __builtin_amdgcn_s_setprio(0);
    f32x16 oacc[4][3];
    set_bases();
    unsigned aO, aR;
    {
      const unsigned ln = lane_now(), hi = ln >> 5, blk = (ln >> 4) & 1, qq = (ln >> 2) & 3, pp = ln & 3;
      aO = L0 + LDS_WO + lw * 12288 + (8 * hi + qq) * 256 + 16 * ((2 * blk + (pp >> 1)) ^ (qq << 2)) + 8 * (pp & 1);
      aR = L0 + LDS_R + (ln & 31) * 512 + 16 * (hi ^ (ln & 15));
    }
    bf16x8 aE[4], aO_[4], bb[3];
    auto rd_a = [&](bf16x8 (&a)[4], int ks) {
      unsigned wa = aO + (ks % 3) * 4096;
      asm volatile("" : "+v"(wa));
#pragma unroll
      for (int nt = 0; nt < 4; ++nt) a[nt] = ldstr(wa ^ (unsigned)(nt << 6), 1024);
    };
    auto rd_b = [&](int qt, int ks) { unsigned ra = aR; asm volatile("" : "+v"(ra)); bb[qt] = lds128((ra ^ ((unsigned)((ks + roto) & 15) << 5)) + qt * 16384); };
    auto ostepx = [&](bool na, int ks, bf16x8 (&cur)[4], bf16x8 (&nxt)[4], bool nb, bool first) {
      __builtin_amdgcn_sched_barrier(0);
      if (na) rd_a(nxt, ks + 1);
#pragma unroll
      for (int qt = 0; qt < 3; ++qt) {
#pragma unroll
        for (int nt = 0; nt < 4; ++nt) {
          if (first) { f32x16 z; for (int i = 0; i < 16; ++i) z[i] = 0.f; oacc[nt][qt] = MFMA32(cur[nt], bb[qt], z); }
          else oacc[nt][qt] = MFMA32(cur[nt], bb[qt], oacc[nt][qt]);
        }
        if (nb) rd_b(qt, ks + 1);
      }
#pragma unroll
      for (int i = 0; i < 4; ++i) {
        __builtin_amdgcn_sched_group_barrier(0x008, 1, 0);
        __builtin_amdgcn_sched_group_barrier(0x002, 1, 0);
        __builtin_amdgcn_sched_group_barrier(0x100, 2, 0);
      }
      __builtin_amdgcn_sched_group_barrier(0x002, 2, 0);
      __builtin_amdgcn_sched_group_barrier(0x100, 1, 0);
      __builtin_amdgcn_sched_group_barrier(0x008, 4, 0);
      __builtin_amdgcn_sched_group_barrier(0x002, 2, 0);
      __builtin_amdgcn_sched_group_barrier(0x100, 1, 0);
      __builtin_amdgcn_sched_group_barrier(0x008, 4, 0);
      __builtin_amdgcn_sched_group_barrier(0x002, 2, 0);
      __builtin_amdgcn_sched_group_barrier(0x100, 1, 0);
      __builtin_amdgcn_sched_barrier(0);
    };
    ld_v(35); attn_store(at, aRw); barrier();
    ld_k(36);
    rd_a(aE, 0);
#pragma unroll
    for (int qt = 0; qt < 3; ++qt) rd_b(qt, 0);
    barrier();
    ld_v(37); ld_wo(3); ostepx(true, 0, aE, aO_, true, true); wait_vmc<12>(); barrier();
    ld_k(38); wait_vmc<12>(); barrier();
    for (int t = 2; t < 14; t += 2) {
      ld_v(35 + 2 * t); ld_wo(t + 2); ostepx(true, t - 1, aO_, aE, true, false); wait_vmc<16>(); barrier();
      ld_k(36 + 2 * t); wait_vmc<12>(); barrier();
      ld_v(37 + 2 * t); ld_wo(t + 3); ostepx(true, t, aE, aO_, true, false); wait_vmc<16>(); barrier();
      ld_k(38 + 2 * t); wait_vmc<12>(); barrier();
    }
    ld_v(63); ld_wo(16); ostepx(true, 13, aO_, aE, true, false); wait_vmc<16>(); barrier();
    wait_vmc<8>(); barrier();
    ld_wo(17); ostepx(true, 14, aE, aO_, true, false); wait_vmc<4>(); barrier();
    ld_wo(18); ostepx(true, 15, aO_, aE, false, false); wait_vmc<4>(); barrier();
    STAMP(3);
    __builtin_amdgcn_s_setprio(PRIO_HI);
#pragma unroll
    for (int qt = 0; qt < 3; ++qt) rd_b(qt, 16);
    for (int ks = 16; ks < 30; ks += 2) {
      ostepx(true, ks, aE, aO_, true, false);
      wait_vmc<0>();
      barrier();
      ostepx(true, ks + 1, aO_, aE, true, false);
      barrier();
    }
    ostepx(true, 30, aE, aO_, true, false);
    barrier();
    ostepx(false, 31, aO_, aE, false, false);
    STAMP(4);
    {
      const __amdgpu_buffer_rsrc_t rsS = __builtin_amdgcn_make_buffer_rsrc((void*)(ws + WS_SLAB + (size_t)((br * 8 + g) * 16 + b) * 98304), 0, 98304, 0x00020000);
      const unsigned ln = lane_now();
#pragma unroll
      for (int nt = 0; nt < 4; ++nt)
#pragma unroll
        for (int qt = 0; qt < 3; ++qt)
#pragma unroll
          for (int pc = 0; pc < 2; ++pc) {
            const f32x16& v = oacc[nt][qt];
            u32x4 w = {pk2(v[8 * pc + 0], v[8 * pc + 1]), pk2(v[8 * pc + 2], v[8 * pc + 3]), pk2(v[8 * pc + 4], v[8 * pc + 5]), pk2(v[8 * pc + 6], v[8 * pc + 7])};
            const unsigned off = (unsigned)((2 * lw + (nt >> 1)) * 12288 + (((nt & 1) * 3 + qt) * 2 + pc) * 1024) + ln * 16;
            __builtin_amdgcn_raw_buffer_store_b128(w, rsS, off, 0, 16);
          }
      asm volatile("s_waitcnt vmcnt(0)" ::: "memory");
    }
    barrier();
    STAMP(5);
    if (wid == 0) {
      const int lane = (int)lane_now();
      const u64 tag = (gen ^ 0x5bd1e9955bd1e995ull) * 0x9E3779B97F4A7C15ull + 0x632be59bd9b4e019ull;
      GAS u64* flags = (GAS u64*)(ws + WS_FLAG);
      if (lane == 0) __hip_atomic_store(flags + (size_t)bid * 16, tag, __ATOMIC_RELAXED, __HIP_MEMORY_SCOPE_AGENT);
      const int gm = lane & 7;
      const int mbid = ((2 * b + (gm & 1)) << 3) | (br * 4 + (gm >> 1));
      for (unsigned spins = 0; spins < (1u << 22); ++spins) {
        const u64 v = __hip_atomic_load(flags + (size_t)mbid * 16, __ATOMIC_RELAXED, __HIP_MEMORY_SCOPE_AGENT);
        if (__all(v == tag)) break;
        __builtin_amdgcn_s_sleep(1);
      }
      if (g == 0 && lane == 0) __hip_atomic_store((GAS u64*)(ws + WS_GEN + (size_t)(br * 16 + b) * 128), gen + 1, __ATOMIC_RELAXED, __HIP_MEMORY_SCOPE_AGENT);
    }
    barrier();
  } else {
    set_bases();
    const unsigned bW0 = l4 * 8192 + 16 * (l15 ^ (l4 << 2)), bW2 = (bW0 ^ 32) + 2 * 31744;
    const unsigned wqs = (unsigned)((lw & 1) * 131072 + (lw >> 1) * 256), wql = L0 + LDS_WQ + lw * 4096;
    auto ld_wq = [&](int u) {
      const int c = (u + rotq) & 15;
      dma4(rsQ, bW0, bW0 + 31744, bW2, bW2 + 31744, (unsigned)(c * 262144 + (u >> 4) * 512) + wqs, wql + (u & 3) * 16384);
    };
    const unsigned bXb = (lane >> 3) * 1024 + 16 * ((lane & 7) ^ l4);
    const unsigned bX0 = bXb ^ (unsigned)(64 * (lw & 1)), bX1 = (bXb ^ (unsigned)(64 * ((lw + 1) & 1))) + 7168, bX2 = bX0 + 2 * 7168;
    auto ld_x = [&](int x) {
      const int cx = (x + (rotq >> 1)) & 7;
      dma3(rsX, bX0, bX1, bX2, (unsigned)(lw * 24576 + cx * 128), L0 + (x < 8 ? xpos(x) : LDS_X2 + (x & 1) * 12288) + lw * 3072);
    };
    f32x4 acc[8][3];
#pragma unroll
    for (int c = 0; c < 8; ++c)
#pragma unroll
      for (int qt = 0; qt < 3; ++qt) acc[c][qt] = f32x4{0.f, 0.f, 0.f, 0.f};
    auto q2step = [&](int c) {
      QFr f;
      q_read(f, aW + (c & 3) * 16384, (aX ^ (unsigned)((c & 1) << 6)) + LDS_X2 + ((c >> 1) & 1) * 12288);
      __builtin_amdgcn_sched_barrier(0);
      q_mma(acc, f);
      __builtin_amdgcn_sched_barrier(0);
    };
    {
      int vc = 0, mw[20], mx[10];
      ld_x(0); vc += 3; mx[0] = vc; ld_wq(0); vc += 4; mw[0] = vc; ld_wq(1); vc += 4; mw[1] = vc; ld_x(1); vc += 3; mx[1] = vc; ld_wq(2); vc += 4; mw[2] = vc;
      wait_k(vc - mw[0]);
      barrier();
#pragma unroll
      for (int p = 0; p <= 16; ++p) {
        ld_wq(p + 3); vc += 4; mw[p + 3] = vc;
        if (p <= 1) {
#pragma unroll
          for (int k = 0; k < 3; ++k) { ld_x(2 + 3 * p + k); vc += 3; mx[2 + 3 * p + k] = vc; }
        }
        if (p == 4) { ld_x(8); vc += 3; mx[8] = vc; }
        if (p == 6) { ld_x(9); vc += 3; mx[9] = vc; }
        if (p == 10) { ld_k(0); vc += 4; }
        if (p == 12) { ld_v(1); vc += 4; }
        if (p == 14) { ld_k(2); vc += 4; }
        if (p == 16) q2step(0);
        BSTAMP(128 + 3 * p);
        if (p + 1 <= 15) {
          const int a = mw[p + 1], c = mx[(p + 1) >> 1];
          wait_k(vc - (a > c ? a : c));
        } else if (p == 15) {
          wait_k(vc - mw[16]);
        }
        BSTAMP(129 + 3 * p);
        barrier();
        BSTAMP(130 + 3 * p);
      }
    }
    STAMP(1);
    bf16x8 qf[3][4];
    for (int m = 0; m < 16; ++m) {
      ld_v(2 * m + 3);
      if (m <= 11) { ld_wq(20 + m); if (m & 1) ld_x(8 + ((m + 3) >> 1)); }
      if (m == 15) { ld_wo(0); ld_wo(1); q_finish(acc, qf); }
      if (m == 0) wait_vmc<20>(); else if (m <= 11) wait_vmc<19>(); else if (m == 12) wait_vmc<15>(); else if (m < 15) wait_vmc<8>(); else wait_vmc<16>();
      barrier();
      ld_k(2 * m + 4);
      if (m == 15) ld_wo(2); else q2step(m + 1);
      if (m == 0) wait_vmc<20>(); else if (m <= 11) { if (m & 1) wait_vmc<15>(); else wait_vmc<12>(); } else if (m < 15) wait_vmc<8>(); else wait_vmc<20>();
      barrier();
    }
    STAMP(2);
    __builtin_amdgcn_s_setprio(PRIO_HI);
    Attn at;
#pragma unroll
    for (int dt = 0; dt < 8; ++dt)
#pragma unroll
      for (int qt = 0; qt < 3; ++qt) at.o[dt][qt] = f32x4{0.f, 0.f, 0.f, 0.f};
#pragma unroll
    for (int qt = 0; qt < 3; ++qt) { at.m[qt] = 0.f; at.l[qt] = 0.f; }
    for (int t = 0; t < 16; ++t) {
      k_step(at, qf, aK + ((2 * t) & 3) * 16384, t ? THR : -__builtin_inff(), t ? 0.f : -__builtin_inff());
      if (t == 0) wait_vmc<12>(); else if (t == 1) wait_vmc<0>();
      FSTAMP(40 + 4 * t);
      barrier();
      FSTAMP(41 + 4 * t);
      v_step(at, aV + ((2 * t + 1) & 3) * 16384);
      if (t == 0) wait_vmc<4>();
      FSTAMP(42 + 4 * t);
      if (t == 15) attn_store(at, aRw);
      barrier();
      FSTAMP(43 + 4 * t);
    }
    STAMP(3);
    __builtin_amdgcn_s_setprio(0);
    set_bases();
    for (int ks = 16; ks < 31; ++ks) {
      if (ks + 3 < 32) { ld_wo(ks + 3); wait_vmc<4>(); } else wait_vmc<0>();
      barrier();
    }
    barrier();
    STAMP(5);
    barrier();
  }
  STAMP(6);
  if (wid < 6) {
    const int lane = (int)lane_now();
    const __amdgpu_buffer_rsrc_t rsS = __builtin_amdgcn_make_buffer_rsrc((void*)(ws + WS_SLAB + (size_t)(br * 8 * 16 + b) * 98304), 0, 8 * 16 * 98304, 0x00020000);
    u32x4 r[8][2];
#pragma unroll
    for (int s = 0; s < 8; ++s)
#pragma unroll
      for (int pc = 0; pc < 2; ++pc)
        r[s][pc] = __builtin_amdgcn_raw_buffer_load_b128(rsS, (unsigned)(s * 16 * 98304 + g * 12288 + (wid * 2 + pc) * 1024) + lane * 16, 0, 16);
    float v[16];
#pragma unroll
    for (int i = 0; i < 16; ++i) v[i] = 0.f;
#pragma unroll
    for (int s = 0; s < 8; ++s)
#pragma unroll
      for (int pc = 0; pc < 2; ++pc)
#pragma unroll
        for (int w = 0; w < 4; ++w) {
          v[8 * pc + 2 * w] += __uint_as_float(r[s][pc][w] << 16);
          v[8 * pc + 2 * w + 1] += __uint_as_float(r[s][pc][w] & 0xffff0000u);
        }
    const int ntl = wid / 3, qt = wid % 3, hi = lane >> 5;
    char* orow = out + (size_t)(b * 96 + qt * 32 + (lane & 31)) * 2048 + (br * 512 + g * 64 + ntl * 32 + 4 * hi) * 2;
#pragma unroll
    for (int gi = 0; gi < 4; ++gi) {
      u32x2 w = {pk2(v[4 * gi], v[4 * gi + 1]), pk2(v[4 * gi + 2], v[4 * gi + 3])};
      *(u32x2*)(orow + gi * 16) = w;
    }
  }
  STAMP(7);
}
}

extern "C" void kernel_launch(void* const* d_in, const int* in_sizes, int n_in, void* d_out, int out_size, void* d_ws, size_t ws_size, hipStream_t stream) {
  fk::fused_kernel<<<256, 512, 0, stream>>>((const char*)d_in[0], (const char*)d_in[1], (const char*)d_in[2], (const char*)d_in[3], (const char*)d_in[4],
                                            (const char*)d_in[5], (const char*)d_in[6], (const char*)d_in[7], (const char*)d_in[8], (const char*)d_in[9],
                                            (char*)d_out, (char*)d_ws);
}
```

```cpp
#include <hip/hip_runtime.h>
#include <hip/hip_bf16.h>
#include <stdint.h>

namespace fk {
typedef __attribute__((ext_vector_type(8))) __bf16 bf16x8;
typedef __attribute__((ext_vector_type(2))) __bf16 bf16x2;
typedef __attribute__((ext_vector_type(4))) short s16x4;
typedef __attribute__((ext_vector_type(8))) short s16x8;
typedef __attribute__((ext_vector_type(2))) float f32x2;
typedef __attribute__((ext_vector_type(4))) float f32x4;
typedef __attribute__((ext_vector_type(16))) float f32x16;
typedef __attribute__((ext_vector_type(4))) unsigned u32x4;
typedef __attribute__((ext_vector_type(2))) unsigned u32x2;
typedef unsigned long long u64;
#define LAS __attribute__((address_space(3)))
#define GAS __attribute__((address_space(1)))
#define DI __device__ __forceinline__

constexpr unsigned LDS_KV = 0;
constexpr unsigned LDS_X1 = 0;
constexpr unsigned LDS_X2 = 65536;
constexpr unsigned LDS_WQ = 98304;
constexpr unsigned LDS_R  = 65536;
constexpr unsigned LDS_WO = 114688;
constexpr unsigned LDS_BYTES = 163840;
constexpr float QSCALE = 0.08838834764831845f * 1.4426950408889634f;
constexpr float THR = 8.0f;
constexpr size_t WS_FLAG = 0;
constexpr size_t WS_GEN  = 32768;
constexpr size_t WS_SLAB = 2097152;

DI unsigned pk2(float a, float b) { f32x2 v = {a, b}; bf16x2 r = __builtin_convertvector(v, bf16x2); return __builtin_bit_cast(unsigned, r); }
DI bf16x8 mk8(unsigned a, unsigned b, unsigned c, unsigned d) { u32x4 w = {a, b, c, d}; return __builtin_bit_cast(bf16x8, w); }
DI bf16x8 lds128(unsigned a) { return *(const LAS bf16x8*)a; }
DI bf16x8 ldstr(unsigned a, unsigned off2) {
  s16x4 lo = __builtin_amdgcn_ds_read_tr16_b64_v4i16((LAS s16x4*)a);
  s16x4 hi = __builtin_amdgcn_ds_read_tr16_b64_v4i16((LAS s16x4*)(a + off2));
  s16x8 v = __builtin_shufflevector(lo, hi, 0, 1, 2, 3, 4, 5, 6, 7);
  return __builtin_bit_cast(bf16x8, v);
}
#define MFMA16(a, b, c) __builtin_amdgcn_mfma_f32_16x16x32_bf16((a), (b), (c), 0, 0, 0)
#define MFMA32(a, b, c) __builtin_amdgcn_mfma_f32_32x32x16_bf16((a), (b), (c), 0, 0, 0)

#define FK_DMA4(NT) \
  asm volatile("s_mov_b32 m0, %0\n\ts_nop 3\n\t" \
               "buffer_load_dwordx4 %1, %5, %6 offen " NT "lds\n\t" \
               "buffer_load_dwordx4 %2, %5, %6 offen offset:1024 " NT "lds\n\t" \
               "buffer_load_dwordx4 %3, %5, %6 offen offset:2048 " NT "lds\n\t" \
               "buffer_load_dwordx4 %4, %5, %6 offen offset:3072 " NT "lds" \
               :: "s"(lds), "v"(v0), "v"(v1), "v"(v2), "v"(v3), "s"(rs), "s"(soff) : "memory")
DI void dma4(__amdgpu_buffer_rsrc_t rs, unsigned v0, unsigned v1, unsigned v2, unsigned v3, unsigned soff, unsigned lds) { FK_DMA4(""); }
DI void dma4_nt(__amdgpu_buffer_rsrc_t rs, unsigned v0, unsigned v1, unsigned v2, unsigned v3, unsigned soff, unsigned lds) { FK_DMA4("nt "); }
#undef FK_DMA4
DI void dma3(__amdgpu_buffer_rsrc_t rs, unsigned v0, unsigned v1, unsigned v2, unsigned soff, unsigned lds) {
  asm volatile("s_mov_b32 m0, %0\n\ts_nop 3\n\t"
               "buffer_load_dwordx4 %1, %4, %5 offen lds\n\t"
               "buffer_load_dwordx4 %2, %4, %5 offen offset:1024 lds\n\t"
               "buffer_load_dwordx4 %3, %4, %5 offen offset:2048 lds"
               :: "s"(lds), "v"(v0), "v"(v1), "v"(v2), "s"(rs), "s"(soff) : "memory");
}
template <int N> DI void wait_vmc() { asm volatile("s_waitcnt vmcnt(%0)" :: "n"(N) : "memory"); }
DI unsigned lane_now() {
  unsigned l; asm volatile("v_mbcnt_lo_u32_b32 %0, -1, 0\n\tv_mbcnt_hi_u32_b32 %0, -1, %0" : "=v"(l)); return l;
}
DI void barrier() {
  __builtin_amdgcn_sched_barrier(0);
  __builtin_amdgcn_s_waitcnt(0xC07F);
  asm volatile("" ::: "memory");
  __builtin_amdgcn_s_barrier();
  asm volatile("" ::: "memory");
  __builtin_amdgcn_sched_barrier(0);
}
DI void barrier_l1() {
  __builtin_amdgcn_sched_barrier(0);
  __builtin_amdgcn_s_waitcnt(0xC17F);
  asm volatile("" ::: "memory");
  __builtin_amdgcn_s_barrier();
  asm volatile("" ::: "memory");
  __builtin_amdgcn_sched_barrier(0);
}
DI float max3(float a, float b, float c) { return __builtin_elementwise_maximum(__builtin_elementwise_maximum(a, b), c); }
#define WC(k) case k: wait_vmc<k>(); return;
DI void wait_k(int n) {
  switch (n) {
    WC(0) WC(1) WC(2) WC(3) WC(4) WC(5) WC(6) WC(7) WC(8) WC(9) WC(10) WC(11) WC(12) WC(13) WC(14) WC(15)
    WC(16) WC(17) WC(18) WC(19) WC(20) WC(21) WC(22) WC(23) WC(24) WC(25) WC(26) WC(27) WC(28) WC(29) WC(30) WC(31)
    WC(32) WC(33) WC(34) WC(35) WC(36) WC(37) WC(38) WC(39) WC(40)
    default: wait_vmc<40>(); return;
  }
}
#undef WC

template <int NM, int NR> DI void mix_mfma_reads() {
#pragma unroll
  for (int i = 0; i < NR; ++i) {
    __builtin_amdgcn_sched_group_barrier(0x008, 1, 0);
    __builtin_amdgcn_sched_group_barrier(0x002, 1, 0);
    __builtin_amdgcn_sched_group_barrier(0x100, 1, 0);
  }
  __builtin_amdgcn_sched_group_barrier(0x008, NM - NR, 0);
}
struct QFr { bf16x8 a[8]; bf16x8 b[3]; };
DI void q_read(QFr& f, unsigned wa, unsigned xa) {
#pragma unroll
  for (int c = 0; c < 8; ++c) f.a[c] = ldstr(wa ^ (unsigned)(c << 5), 1024);
#pragma unroll
  for (int qt = 0; qt < 3; ++qt) f.b[qt] = lds128(xa + qt * 2048);
}
DI void q_mma(f32x4 (&acc)[8][3], const QFr& f) {
#pragma unroll
  for (int c = 0; c < 8; ++c)
#pragma unroll
    for (int qt = 0; qt < 3; ++qt) acc[c][qt] = MFMA16(f.a[c], f.b[qt], acc[c][qt]);
}
DI void q_finish(const f32x4 (&acc)[8][3], bf16x8 (&qf)[3][4]) {
#pragma unroll
  for (int qt = 0; qt < 3; ++qt)
#pragma unroll
    for (int s = 0; s < 4; ++s) {
      const f32x4 ta = acc[2 * s][qt], tb = acc[2 * s + 1][qt];
      unsigned a0 = pk2(ta[0] * QSCALE, ta[1] * QSCALE), a1 = pk2(ta[2] * QSCALE, ta[3] * QSCALE);
      unsigned b0 = pk2(tb[0] * QSCALE, tb[1] * QSCALE), b1 = pk2(tb[2] * QSCALE, tb[3] * QSCALE);
      auto r0 = __builtin_amdgcn_permlane32_swap(a0, b0, false, false);
      auto r1 = __builtin_amdgcn_permlane32_swap(a1, b1, false, false);
      auto s0 = __builtin_amdgcn_permlane16_swap(r0[0], r0[1], false, false);
      auto s1 = __builtin_amdgcn_permlane16_swap(r1[0], r1[1], false, false);
      qf[qt][s] = mk8(s0[0], s1[0], s0[1], s1[1]);
    }
}

struct Attn {
  f32x4 o[8][3];
  f32x4 nm, l;
};
typedef f32x4 STile[2][3];
DI float xmax4(float v) {
  v = __builtin_elementwise_maximum(v, __shfl_xor(v, 16)); return __builtin_elementwise_maximum(v, __shfl_xor(v, 32));
}
DI void qk(STile& s, const Attn& at, const bf16x8 (&qf)[3][4], unsigned ka) {
#pragma unroll
  for (int kt = 0; kt < 2; ++kt) {
    bf16x8 kf[4];
#pragma unroll
    for (int ks = 0; ks < 4; ++ks) kf[ks] = lds128((ka ^ (unsigned)(ks << 6)) + kt * 4096);
#pragma unroll
    for (int qt = 0; qt < 3; ++qt) {
      const float nm = at.nm[qt];
      f32x4 c = {nm, nm, nm, nm};
#pragma unroll
      for (int ks = 0; ks < 4; ++ks) c = MFMA16(kf[ks], qf[qt][ks], c);
      s[kt][qt] = c;
    }
  }
}
DI void maxfix(STile& s, Attn& at, bool first) {
  float mx[3];
#pragma unroll
  for (int qt = 0; qt < 3; ++qt) {
    const f32x4 a = s[0][qt], b = s[1][qt];
    mx[qt] = max3(max3(max3(a[0], a[1], a[2]), a[3], b[0]), max3(b[1], b[2], b[3]), a[0]);
  }
  const float mall = max3(mx[0], mx[1], mx[2]);
  if (first || __any(mall > THR)) {
#pragma unroll
    for (int qt = 0; qt < 3; ++qt) {
      float d = xmax4(mx[qt]);
      if (!first) {
        d = __builtin_elementwise_maximum(d, 0.f);
        const float f = __builtin_amdgcn_exp2f(-d);
        at.l[qt] *= f;
#pragma unroll
        for (int dt = 0; dt < 8; ++dt) at.o[dt][qt] *= f;
      }
      at.nm[qt] -= d;
      s[0][qt] -= d; s[1][qt] -= d;
    }
  }
}
DI void expsum(const STile& s, Attn& at, bf16x8 (&p)[3]) {
#pragma unroll
  for (int qt = 0; qt < 3; ++qt) {
    float e[8];
#pragma unroll
    for (int i = 0; i < 4; ++i) { e[i] = __builtin_amdgcn_exp2f(s[0][qt][i]); e[4 + i] = __builtin_amdgcn_exp2f(s[1][qt][i]); }
    at.l[qt] += ((e[0] + e[1]) + (e[2] + e[3])) + ((e[4] + e[5]) + (e[6] + e[7]));
    p[qt] = mk8(pk2(e[0], e[1]), pk2(e[2], e[3]), pk2(e[4], e[5]), pk2(e[6], e[7]));
  }
}
DI void pv(Attn& at, const bf16x8 (&p)[3], unsigned va) {
  asm volatile("" : "+v"(va));
  bf16x8 vf[8];
#pragma unroll
  for (int dt = 0; dt < 8; ++dt) vf[dt] = ldstr(va ^ (unsigned)(dt << 5), 4096);
#pragma unroll
  for (int dt = 0; dt < 8; ++dt)
#pragma unroll
    for (int qt = 0; qt < 3; ++qt) at.o[dt][qt] = MFMA16(vf[dt], p[qt], at.o[dt][qt]);
}
DI void hint_x() {
  __builtin_amdgcn_sched_group_barrier(0x002, 4, 0);
  __builtin_amdgcn_sched_group_barrier(0x100, 4, 0);
  __builtin_amdgcn_sched_group_barrier(0x400, 6, 0);
  __builtin_amdgcn_sched_group_barrier(0x002, 6, 0);
#pragma unroll
  for (int i = 0; i < 24; ++i) {
    __builtin_amdgcn_sched_group_barrier(0x008, 1, 0);
    if (i >= 6 && i < 10) __builtin_amdgcn_sched_group_barrier(0x100, 1, 0);
    if (i < 18) __builtin_amdgcn_sched_group_barrier(0x400, 1, 0);
    __builtin_amdgcn_sched_group_barrier(0x002, 2, 0);
  }
}
DI void hint_y() {
#pragma unroll
  for (int i = 0; i < 8; ++i) {
    __builtin_amdgcn_sched_group_barrier(0x002, 1, 0);
    __builtin_amdgcn_sched_group_barrier(0x100, 2, 0);
  }
  __builtin_amdgcn_sched_group_barrier(0x002, 4, 0);
#pragma unroll
  for (int i = 0; i < 10; ++i) {
    __builtin_amdgcn_sched_group_barrier(0x008, 2, 0);
    __builtin_amdgcn_sched_group_barrier(0x002, 1, 0);
  }
  __builtin_amdgcn_sched_group_barrier(0x008, 4, 0);
}
template <class H> DI void attention(Attn& at, const bf16x8 (&qf)[3][4], unsigned aK, unsigned aV, H pre) {
#pragma unroll
  for (int dt = 0; dt < 8; ++dt)
#pragma unroll
    for (int qt = 0; qt < 3; ++qt) at.o[dt][qt] = f32x4{0.f, 0.f, 0.f, 0.f};
  at.nm = f32x4{0.f, 0.f, 0.f, 0.f}; at.l = f32x4{0.f, 0.f, 0.f, 0.f};
  STile s0, s1;
  bf16x8 p[3];
  qk(s0, at, qf, aK);
  maxfix(s0, at, true);
  pre(0); barrier();
  for (int t = 0; t < 14; t += 2) {
    const unsigned sl = (unsigned)((2 * t) & 3) * 16384;
    qk(s1, at, qf, aK + sl + 16384); expsum(s0, at, p); hint_x();
    pre(1);
    barrier();
    pv(at, p, aV + sl + 32768); hint_y(); maxfix(s1, at, false);
    barrier();
    qk(s0, at, qf, aK + sl + 49152); expsum(s1, at, p); hint_x();
    barrier();
    pv(at, p, aV + sl); hint_y(); maxfix(s0, at, false);
    barrier();
  }
  qk(s1, at, qf, aK + 16384); expsum(s0, at, p); hint_x();
  barrier();
  pv(at, p, aV + 32768); hint_y(); maxfix(s1, at, false);
  barrier();
  expsum(s1, at, p);
  pv(at, p, aV + 49152);
}
DI void attn_store(Attn& at, unsigned ra) {
#pragma unroll
  for (int qt = 0; qt < 3; ++qt) {
    float l = at.l[qt]; l += __shfl_xor(l, 16); l += __shfl_xor(l, 32);
    const float inv = 1.0f / l;
#pragma unroll
    for (int dt = 0; dt < 8; ++dt) {
      const f32x4 v = at.o[dt][qt] * inv;
      u32x2 w = {pk2(v[0], v[1]), pk2(v[2], v[3])};
      *(LAS u32x2*)((ra ^ (unsigned)(dt << 5)) + qt * 8192) = w;
    }
  }
}

__global__ __launch_bounds__(512, 2) void fused_kernel(
    const char* __restrict__ xA, const char* __restrict__ kA, const char* __restrict__ vA, const char* __restrict__ wqA, const char* __restrict__ woA,
    const char* __restrict__ xB, const char* __restrict__ kB, const char* __restrict__ vB, const char* __restrict__ wqB, const char* __restrict__ woB,
    char* __restrict__ out, char* __restrict__ ws) {
  __shared__ __attribute__((aligned(1024))) char smem[LDS_BYTES];
  const unsigned L0 = (unsigned)(uintptr_t)(LAS char*)smem;
  const int tid = threadIdx.x;
  const int wid = __builtin_amdgcn_readfirstlane(tid >> 6);
  const int bid = blockIdx.x, xcd = bid & 7, jj = bid >> 3;
  const int br = xcd >> 2, g = (xcd & 3) * 2 + (jj & 1), b = jj >> 1;
  const int grp = wid >> 2, lw = wid & 3, hh = lw >> 1, jq = lw & 1;
  const int rot = (bid * 5 + (bid >> 3)) & 15, rotq = 2 * ((b + 4 * (g & 1)) & 7), roto = b;
  const __amdgpu_buffer_rsrc_t rsK = __builtin_amdgcn_make_buffer_rsrc((void*)((br ? kB : kA) + (size_t)(b * 32 + g * 4) * 131072), 0, 524288, 0x00020000);
  const __amdgpu_buffer_rsrc_t rsV = __builtin_amdgcn_make_buffer_rsrc((void*)((br ? vB : vA) + (size_t)(b * 32 + g * 4) * 131072), 0, 524288, 0x00020000);
  const __amdgpu_buffer_rsrc_t rsQ = __builtin_amdgcn_make_buffer_rsrc((void*)((br ? wqB : wqA) + g * 1024), 0, 4194304 - g * 1024, 0x00020000);
  const __amdgpu_buffer_rsrc_t rsO = __builtin_amdgcn_make_buffer_rsrc((void*)((br ? woB : woA) + (size_t)g * 524288), 0, 524288, 0x00020000);
  const __amdgpu_buffer_rsrc_t rsX = __builtin_amdgcn_make_buffer_rsrc((void*)((br ? xB : xA) + (size_t)b * 98304), 0, 98304, 0x00020000);

  unsigned bK, bV, bO;
  auto set_bases = [&]() {
    const unsigned ln = lane_now(), n4 = ln >> 4, n15 = ln & 15;
    bK = n4 * 256 + 16 * (n15 ^ n4); bV = n4 * 256 + 16 * (n15 ^ (2 * n4)); bO = n4 * 1024 + 16 * (n15 ^ (n4 << 2));
  };
  const unsigned kvq = (unsigned)(hh * 131072 + jq * 4096), kvl = L0 + LDS_KV + lw * 4096;
  auto ld_kt = [&](int i, int tile) {
    const int t = (tile + rot) & 15;
    unsigned v = bK; asm volatile("" : "+v"(v));
    dma4_nt(rsK, v, v ^ 64, v ^ 128, v ^ 192, (unsigned)((i >> 5) * 262144 + t * 8192) + kvq, kvl + (i & 3) * 16384);
  };
  auto ld_vt = [&](int i, int tile) {
    const int t = (tile + rot) & 15;
    unsigned v = bV; asm volatile("" : "+v"(v));
    dma4_nt(rsV, v, v ^ 128, v, v ^ 128, (unsigned)((i >> 5) * 262144 + t * 8192) + kvq, kvl + (i & 3) * 16384);
  };
  auto ld_odd = [&](int i) { const int j = i & 31; if (j == 31) ld_vt(i, 15); else ld_kt(i, (j + 1) >> 1); };
  auto ld_even = [&](int i) { const int j = i & 31; if (j == 0) ld_kt(i, 0); else ld_vt(i, (j - 2) >> 1); };
  auto wo_loc = [&](int ks) -> unsigned {
    if (ks < 19) return LDS_WO + lw * 12288 + (ks % 3) * 4096;
    const int idx = (ks - 16) % 7;
    return idx < 3 ? LDS_WO + lw * 12288 + ((idx + 1) % 3) * 4096 : LDS_KV + (idx - 3) * 16384 + lw * 4096;
  };
  auto ld_wo = [&](int ks) {
    const int kk = (ks + roto) & 15;
    unsigned v = bO; asm volatile("" : "+v"(v));
    dma4(rsO, v, v + 3072, v + 2 * 3072, v + 3 * 3072, (unsigned)(((ks >> 4) * 256 + kk * 16) * 1024 + lw * 256), L0 + wo_loc(ks));
  };
#define FK_LANE_ADDRS \
  const unsigned ln_ = lane_now(), l4 = ln_ >> 4, l15 = ln_ & 15, q2 = (ln_ >> 2) & 3, p2 = ln_ & 3; \
  const unsigned aW = L0 + LDS_WQ + hh * 8192 + (8 * l4 + q2) * 256 + 16 * ((p2 >> 1) ^ (q2 << 2) ^ ((l4 & 1) << 1)) + 8 * (p2 & 1); \
  const unsigned aX = L0 + (48 * jq + l15) * 128 + 16 * (l4 ^ (l15 >> 1));
#define FK_KV_ADDRS \
  unsigned aK, aV; \
  { const unsigned ln = lane_now(), n4 = ln >> 4, n15 = ln & 15, qq = (ln >> 2) & 3, pp = ln & 3; \
    aK = L0 + LDS_KV + hh * 8192 + n15 * 256 + 16 * (n4 ^ n15); \
    aV = L0 + LDS_KV + hh * 8192 + (4 * n4 + qq) * 256 + 16 * ((pp >> 1) ^ (((4 * n4 + qq) & 7) << 1)) + 8 * (pp & 1); }
  auto aRw_now = [&]() { const unsigned ln = lane_now(), n4 = ln >> 4, n15 = ln & 15; return L0 + LDS_R + (48 * jq + n15) * 512 + hh * 256 + 16 * ((n4 >> 1) ^ n15) + 8 * (n4 & 1); };

  auto xpos = [](int x) { return (unsigned)(LDS_X1 + x * 12288); };

  if (grp == 0) {
    FK_LANE_ADDRS
    u64 gen = 0;
    __builtin_amdgcn_s_setprio(1);
    if (wid == 0) gen = __hip_atomic_load((GAS u64*)(ws + WS_GEN + (size_t)(br * 16 + b) * 128), __ATOMIC_RELAXED, __HIP_MEMORY_SCOPE_AGENT);
    bf16x8 qf[3][4];
    {
      f32x4 acc[8][3];
#pragma unroll
      for (int c = 0; c < 8; ++c)
#pragma unroll
        for (int qt = 0; qt < 3; ++qt) acc[c][qt] = f32x4{0.f, 0.f, 0.f, 0.f};
      QFr f0, f1;
      barrier();
      q_read(f0, aW, aX + xpos(0));
      barrier();
      auto qstep = [&](QFr& rd, const QFr& mm, unsigned wslot, unsigned xa) {
        q_read(rd, aW + wslot * 16384, xa);
        q_mma(acc, mm);
        mix_mfma_reads<24, 19>();
        barrier();
      };
      const unsigned aX1 = aX ^ 64u;
      for (int p = 1; p < 13; p += 4) {
        const unsigned xo = (unsigned)(p >> 1) * 12288;
        qstep(f1, f0, 1, aX1 + xo);
        qstep(f0, f1, 2, aX + xo + 12288);
        qstep(f1, f0, 3, aX1 + xo + 12288);
        qstep(f0, f1, 0, aX + xo + 24576);
      }
      qstep(f1, f0, 1, aX1 + 6 * 12288);
      qstep(f0, f1, 2, aX + 7 * 12288);
      q_read(f1, aW + 3 * 16384, aX1 + 7 * 12288);
      q_mma(acc, f0);
      mix_mfma_reads<24, 19>();
      barrier();
      q_mma(acc, f1);
      q_finish(acc, qf);
      barrier();
    }
    FK_KV_ADDRS
    Attn at;
    attention(at, qf, aK, aV, [](int) {});
    barrier();
    __builtin_amdgcn_s_setprio(0);
    f32x16 oacc[4][3];
    set_bases();
    unsigned aO, aR;
    {
      const unsigned ln = lane_now(), hi = ln >> 5, blk = (ln >> 4) & 1, qq = (ln >> 2) & 3, pp = ln & 3;
      aO = L0 + (8 * hi + qq) * 256 + 16 * ((2 * blk + (pp >> 1)) ^ (qq << 2)) + 8 * (pp & 1);
      aR = L0 + LDS_R + (ln & 31) * 512 + 16 * (hi ^ (ln & 15));
    }
    bf16x8 aE[4], aO_[4], bb[3];
    auto rd_a = [&](bf16x8 (&a)[4], int ks) {
      unsigned wa = aO + wo_loc(ks);
      asm volatile("" : "+v"(wa));
#pragma unroll
      for (int nt = 0; nt < 4; ++nt) a[nt] = ldstr(wa ^ (unsigned)(nt << 6), 1024);
    };
    auto rd_b = [&](int qt, int ks) { unsigned ra = aR; asm volatile("" : "+v"(ra)); bb[qt] = lds128((ra ^ ((unsigned)((ks + roto) & 15) << 5)) + qt * 16384); };
    auto ostepx = [&](bool na, int ks, bf16x8 (&cur)[4], bf16x8 (&nxt)[4], bool nb, bool first) {
      __builtin_amdgcn_sched_barrier(0);
      if (na) rd_a(nxt, ks + 1);
#pragma unroll
      for (int qt = 0; qt < 3; ++qt) {
#pragma unroll
        for (int nt = 0; nt < 4; ++nt) {
          if (first) { f32x16 z; for (int i = 0; i < 16; ++i) z[i] = 0.f; oacc[nt][qt] = MFMA32(cur[nt], bb[qt], z); }
          else oacc[nt][qt] = MFMA32(cur[nt], bb[qt], oacc[nt][qt]);
        }
        if (nb) rd_b(qt, ks + 1);
      }
#pragma unroll
      for (int i = 0; i < 4; ++i) {
        __builtin_amdgcn_sched_group_barrier(0x008, 1, 0);
        __builtin_amdgcn_sched_group_barrier(0x002, 1, 0);
        __builtin_amdgcn_sched_group_barrier(0x100, 2, 0);
      }
      __builtin_amdgcn_sched_group_barrier(0x002, 2, 0);
      __builtin_amdgcn_sched_group_barrier(0x100, 1, 0);
      __builtin_amdgcn_sched_group_barrier(0x008, 4, 0);
      __builtin_amdgcn_sched_group_barrier(0x002, 2, 0);
      __builtin_amdgcn_sched_group_barrier(0x100, 1, 0);
      __builtin_amdgcn_sched_group_barrier(0x008, 4, 0);
      __builtin_amdgcn_sched_group_barrier(0x002, 2, 0);
      __builtin_amdgcn_sched_group_barrier(0x100, 1, 0);
      __builtin_amdgcn_sched_barrier(0);
    };
    auto mm1 = [&](bf16x8 (&cur)[4], int qt, int nt, bool first) {
      __builtin_amdgcn_sched_barrier(0);
      if (first) { f32x16 z; for (int i = 0; i < 16; ++i) z[i] = 0.f; oacc[nt][qt] = MFMA32(cur[nt], bb[qt], z); }
      else oacc[nt][qt] = MFMA32(cur[nt], bb[qt], oacc[nt][qt]);
      __builtin_amdgcn_sched_barrier(0);
    };
    auto gap = [&]() { __builtin_amdgcn_s_sleep(1); };
    auto otile = [&](int k, bf16x8 (&cur)[4], bf16x8 (&nxt)[4], int item, bool first) {
      rd_a(nxt, k + 1);
      mm1(cur, 0, 0, first); ld_wo(k + 3);
      mm1(cur, 0, 1, first); gap();
      mm1(cur, 0, 2, first); ld_odd(item);
      mm1(cur, 0, 3, first); rd_b(0, k + 1); gap();
      mm1(cur, 1, 0, first);
      wait_vmc<12>(); barrier();
      mm1(cur, 1, 1, first); ld_even(item + 1);
      mm1(cur, 1, 2, first); gap();
      mm1(cur, 1, 3, first); rd_b(1, k + 1); gap();
      mm1(cur, 2, 0, first); gap();
      mm1(cur, 2, 1, first); gap();
      mm1(cur, 2, 2, first); gap();
      mm1(cur, 2, 3, first); rd_b(2, k + 1);
      wait_vmc<12>(); barrier();
    };
    auto oslow = [&](int k, bf16x8 (&cur)[4], bf16x8 (&nxt)[4], bool nb) {
      rd_a(nxt, k + 1);
#pragma unroll
      for (int qt = 0; qt < 3; ++qt) {
#pragma unroll
        for (int nt = 0; nt < 4; ++nt) { mm1(cur, qt, nt, false); if (nt == 3 && nb) rd_b(qt, k + 1); gap(); }
      }
    };
    ld_odd(35); attn_store(at, aRw_now()); barrier();
    ld_even(36);
    rd_a(aE, 0);
#pragma unroll
    for (int qt = 0; qt < 3; ++qt) rd_b(qt, 0);
    barrier();
    otile(0, aE, aO_, 37, true);
    for (int t = 2; t < 14; t += 2) {
      otile(t - 1, aO_, aE, 35 + 2 * t, false);
      otile(t, aE, aO_, 37 + 2 * t, false);
    }
    ld_wo(16); ld_odd(63); oslow(13, aO_, aE, true); wait_vmc<12>(); barrier();
    ld_wo(19); wait_vmc<12>(); barrier();
    ld_wo(17); ld_wo(20); oslow(14, aE, aO_, true); wait_vmc<12>(); barrier();
    ld_wo(18); ld_wo(21); oslow(15, aO_, aE, false); wait_vmc<12>(); barrier();
    __builtin_amdgcn_s_setprio(1);
#pragma unroll
    for (int qt = 0; qt < 3; ++qt) rd_b(qt, 16);
#pragma unroll
    for (int ks = 16; ks < 30; ks += 2) {
      ostepx(true, ks, aE, aO_, true, false);
      if (ks == 16) wait_vmc<0>();
      barrier_l1();
      ostepx(true, ks + 1, aO_, aE, true, false);
      barrier_l1();
    }
    ostepx(true, 30, aE, aO_, true, false);
    barrier_l1();
    ostepx(false, 31, aO_, aE, false, false);
    {
      const __amdgpu_buffer_rsrc_t rsS = __builtin_amdgcn_make_buffer_rsrc((void*)(ws + WS_SLAB + (size_t)((br * 8 + g) * 16 + b) * 98304), 0, 98304, 0x00020000);
      const unsigned ln = lane_now();
#pragma unroll
      for (int nt = 0; nt < 4; ++nt)
#pragma unroll
        for (int qt = 0; qt < 3; ++qt)
#pragma unroll
          for (int pc = 0; pc < 2; ++pc) {
            const f32x16& v = oacc[nt][qt];
            u32x4 w = {pk2(v[8 * pc + 0], v[8 * pc + 1]), pk2(v[8 * pc + 2], v[8 * pc + 3]), pk2(v[8 * pc + 4], v[8 * pc + 5]), pk2(v[8 * pc + 6], v[8 * pc + 7])};
            const unsigned pofs = (unsigned)((((nt & 1) * 3 + qt) * 2 + pc) * 1024) + ln * 16;
            if (2 * lw + (nt >> 1) == g) *(LAS u32x4*)(L0 + pofs) = w;
            else __builtin_amdgcn_raw_buffer_store_b128(w, rsS, (unsigned)((2 * lw + (nt >> 1)) * 12288) + pofs, 0, 16);
          }
      asm volatile("s_waitcnt vmcnt(0)" ::: "memory");
    }
    barrier();
    if (wid == 0) {
      const int lane = (int)lane_now();
      const u64 tag = (gen ^ 0x5bd1e9955bd1e995ull) * 0x9E3779B97F4A7C15ull + 0x632be59bd9b4e019ull;
      GAS u64* flags = (GAS u64*)(ws + WS_FLAG);
      if (lane == 0) __hip_atomic_store(flags + (size_t)bid * 16, tag, __ATOMIC_RELAXED, __HIP_MEMORY_SCOPE_AGENT);
      const int gm = lane & 7;
      const int mbid = ((2 * b + (gm & 1)) << 3) | (br * 4 + (gm >> 1));
      for (unsigned spins = 0; spins < (1u << 18); ++spins) {
        const u64 v = __hip_atomic_load(flags + (size_t)mbid * 16, __ATOMIC_RELAXED, __HIP_MEMORY_SCOPE_AGENT);
        if (__all(v == tag)) break;
        __builtin_amdgcn_s_sleep(1);
      }
      if (g == 0 && lane == 0) __hip_atomic_store((GAS u64*)(ws + WS_GEN + (size_t)(br * 16 + b) * 128), gen + 1, __ATOMIC_RELAXED, __HIP_MEMORY_SCOPE_AGENT);
    }
    barrier();
  } else {
    FK_LANE_ADDRS
    set_bases();
    const unsigned bW0 = l4 * 8192 + 16 * (l15 ^ (l4 << 2)), bW2 = (bW0 ^ 32) + 2 * 31744;
    const unsigned wqs = (unsigned)((lw & 1) * 131072 + (lw >> 1) * 256), wql = L0 + LDS_WQ + lw * 4096;
    auto ld_wq = [&](int u) {
      const int c = (u + rotq) & 15;
      dma4(rsQ, bW0, bW0 + 31744, bW2, bW2 + 31744, (unsigned)(c * 262144 + (u >> 4) * 512) + wqs, wql + (u & 3) * 16384);
    };
    const unsigned bXb = (ln_ >> 3) * 1024 + 16 * ((ln_ & 7) ^ l4);
    const unsigned bX0 = bXb ^ (unsigned)(64 * (lw & 1)), bX1 = (bXb ^ (unsigned)(64 * ((lw + 1) & 1))) + 7168, bX2 = bX0 + 2 * 7168;
    auto ld_x = [&](int x) {
      const int cx = (x + (rotq >> 1)) & 7;
      dma3(rsX, bX0, bX1, bX2, (unsigned)(lw * 24576 + cx * 128), L0 + (x < 8 ? xpos(x) : LDS_X2 + (x & 1) * 12288) + lw * 3072);
    };
    f32x4 acc[8][3];
#pragma unroll
    for (int c = 0; c < 8; ++c)
#pragma unroll
      for (int qt = 0; qt < 3; ++qt) acc[c][qt] = f32x4{0.f, 0.f, 0.f, 0.f};
    QFr g0, g1;
    auto q2read = [&](QFr& f, int c) { q_read(f, aW + (c & 3) * 16384, (aX ^ (unsigned)((c & 1) << 6)) + LDS_X2 + ((c >> 1) & 1) * 12288); };
    auto q2both = [&](QFr& rd, int c, const QFr& mm) {
      __builtin_amdgcn_sched_barrier(0);
      q2read(rd, c);
      q_mma(acc, mm);
      mix_mfma_reads<24, 19>();
      __builtin_amdgcn_sched_barrier(0);
    };
    {
      int vc = 0, mw[20], mx[10];
      ld_x(0); vc += 3; mx[0] = vc; ld_wq(0); vc += 4; mw[0] = vc;
      ld_wq(1); vc += 4; mw[1] = vc; ld_x(1); vc += 3; mx[1] = vc; ld_wq(2); vc += 4; mw[2] = vc;
      wait_k(vc - mw[0]);
      barrier();
#pragma unroll
      for (int p = 0; p <= 16; ++p) {
        ld_wq(p + 3); vc += 4; mw[p + 3] = vc;
        if (p <= 5) { ld_x(2 + p); vc += 3; mx[2 + p] = vc; }
        if (p == 4) { ld_even(0); vc += 4; }
        if (p == 6) { ld_odd(1); vc += 4; }
        if (p == 8) { ld_even(2); vc += 4; }
        if (p == 14) { ld_x(8); vc += 3; mx[8] = vc; }
        if (p == 16) { ld_x(9); vc += 3; mx[9] = vc; }
        if (p == 16) q2read(g0, 0);
        if (p + 1 <= 15) {
          const int a = mw[p + 1], c = mx[(p + 1) >> 1];
          wait_k(vc - (a > c ? a : c));
        } else if (p == 15) {
          wait_k(vc - mx[8]);
        }
        barrier();
      }
    }
    bf16x8 qf[3][4];
    ld_wq(20); ld_odd(3); q2both(g1, 1, g0); wait_vmc<8>(); barrier();
    ld_even(4); q2both(g0, 2, g1); wait_vmc<12>(); barrier();
    for (int m = 1; m < 13; m += 2) {
      if (m <= 11) { ld_wq(20 + m); ld_x(8 + ((m + 3) >> 1)); }
      ld_odd(2 * m + 3);
      wait_vmc<15>(); barrier();
      ld_even(2 * m + 4); q2both(g1, m + 2, g0); wait_vmc<15>(); barrier();
      if (m + 1 <= 11) { ld_wq(21 + m); ld_odd(2 * m + 5); wait_vmc<12>(); } else { ld_odd(2 * m + 5); wait_vmc<8>(); }
      barrier();
      ld_even(2 * m + 6); q2both(g0, m + 3, g1);
      if (m + 1 <= 11) wait_vmc<12>(); else wait_vmc<8>();
      barrier();
    }
    ld_odd(29); wait_vmc<8>(); barrier();
    ld_even(30); q2both(g1, 15, g0); wait_vmc<8>(); barrier();
    ld_odd(31); wait_vmc<8>(); barrier();
    ld_even(32); __builtin_amdgcn_sched_barrier(0); q_mma(acc, g1); __builtin_amdgcn_sched_barrier(0); wait_vmc<8>(); barrier();
    ld_wo(0); ld_wo(1); ld_odd(33); q_finish(acc, qf); wait_vmc<16>(); barrier();
    ld_wo(2); ld_even(34); wait_vmc<20>(); barrier();
    __builtin_amdgcn_s_setprio(1);
    FK_KV_ADDRS
    Attn at;
    attention(at, qf, aK, aV, [](int st) { if (st == 0) wait_vmc<8>(); else wait_vmc<0>(); });
    attn_store(at, aRw_now());
    barrier();
    __builtin_amdgcn_s_setprio(0);
    set_bases();
#pragma unroll
    for (int ks = 16; ks < 31; ++ks) {
      if (ks + 6 < 32) ld_wo(ks + 6);
      if (ks + 2 >= 22) { const int last = ks + 6 < 32 ? ks + 6 : 31; wait_k(4 * (last - (ks + 2) > 0 ? last - (ks + 2) : 0)); }
      barrier();
    }
    barrier();
    barrier();
  }
  if (wid < 6) {
    const int lane = (int)lane_now();
    const __amdgpu_buffer_rsrc_t rsS = __builtin_amdgcn_make_buffer_rsrc((void*)(ws + WS_SLAB + (size_t)(br * 8 * 16 + b) * 98304), 0, 8 * 16 * 98304, 0x00020000);
    u32x4 r[9][2];
#pragma unroll
    for (int pc = 0; pc < 2; ++pc) r[8][pc] = *(const LAS u32x4*)(L0 + (wid * 2 + pc) * 1024 + lane * 16);
#pragma unroll
    for (int s = 0; s < 8; ++s)
#pragma unroll
      for (int pc = 0; pc < 2; ++pc)
        r[s][pc] = __builtin_amdgcn_raw_buffer_load_b128(rsS, (unsigned)(g * 12288 + (wid * 2 + pc) * 1024) + lane * 16, s == g ? 8u * 16u * 98304u : (unsigned)(s * 16 * 98304), 16);
    float v[16];
#pragma unroll
    for (int i = 0; i < 16; ++i) v[i] = 0.f;
#pragma unroll
    for (int s = 0; s < 9; ++s)
#pragma unroll
      for (int pc = 0; pc < 2; ++pc)
#pragma unroll
        for (int w = 0; w < 4; ++w) {
          v[8 * pc + 2 * w] += __uint_as_float(r[s][pc][w] << 16);
          v[8 * pc + 2 * w + 1] += __uint_as_float(r[s][pc][w] & 0xffff0000u);
        }
    const int ntl = wid / 3, qt = wid % 3, hi = lane >> 5;
    char* orow = out + (size_t)(b * 96 + qt * 32 + (lane & 31)) * 2048 + (br * 512 + g * 64 + ntl * 32 + 4 * hi) * 2;
#pragma unroll
    for (int gi = 0; gi < 4; ++gi) {
      u32x2 w = {pk2(v[4 * gi], v[4 * gi + 1]), pk2(v[4 * gi + 2], v[4 * gi + 3])};
      *(u32x2*)(orow + gi * 16) = w;
    }
  }
}
}

extern "C" void kernel_launch(void* const* d_in, const int* in_sizes, int n_in, void* d_out, int out_size, void* d_ws, size_t ws_size, hipStream_t stream) {
  fk::fused_kernel<<<256, 512, 0, stream>>>((const char*)d_in[0], (const char*)d_in[1], (const char*)d_in[2], (const char*)d_in[3], (const char*)d_in[4],
                                            (const char*)d_in[5], (const char*)d_in[6], (const char*)d_in[7], (const char*)d_in[8], (const char*)d_in[9],
                                            (char*)d_out, (char*)d_ws);
}
```
